```python
import torch

def _softmax(x, axis=-1):
    return torch.softmax(x, dim=axis)
def _gelu(x):
    return torch.nn.functional.gelu(x, approximate="tanh")
def _silu(x):
    return torch.nn.functional.silu(x)
def _sigmoid(x):
    return torch.sigmoid(x)
def _layernorm(x, axis=-1, eps=1e-5):
    m = x.mean(dim=axis, keepdim=True)
    v = x.var(dim=axis, keepdim=True, unbiased=False)
    return (x - m) / torch.sqrt(v + eps)
def _scatter_add(tgt, idx, src, axis=0):
    return tgt.clone().index_add_(axis, idx, src)
def _segment_sum(src, seg, n):
    out = torch.zeros((n,) + src.shape[1:], dtype=src.dtype)
    return out.index_add_(0, seg, src)
def _pad(x, pw):
    flat = tuple(v for pair in reversed(pw) for v in pair)
    return torch.nn.functional.pad(x, flat)

kernel_name = 'parallel_sum__compressed_kv_cla__dense_activation_squared_relu__decode'

def setup_inputs():

    _w = (lambda a: (lambda t: t.to(torch.bfloat16) if t.is_floating_point() else t)(torch.as_tensor(a)))
    def _A_setup():
        B, H, Sq, Sk, D, M = 4, 32, 1, 4096, 128, 512
        Dc = 4 * D
        return {
            "x": _w((lambda *s: torch.randn(*s, dtype=torch.bfloat16) * 0.05)(B, Sq, M)),
            "c_kv": _w((lambda *s: torch.randn(*s, dtype=torch.bfloat16) * 3.2)(B, Sk, Dc)),
            "w_q": _w((lambda *s: torch.randn(*s, dtype=torch.bfloat16) * 1.6)(M, H, D) * (M ** -0.5)),
            "w_uk": _w((lambda *s: torch.randn(*s, dtype=torch.bfloat16) * 1.6)(Dc, H, D) * (Dc ** -0.5)),
            "w_uv": _w((lambda *s: torch.randn(*s, dtype=torch.bfloat16) * 3.2)(Dc, H, D) * (Dc ** -0.5)),
            "w_o": _w((lambda *s: torch.randn(*s, dtype=torch.bfloat16) * 3.2)(H, D, M) * ((H * D) ** -0.5)),
        }
    _A_in = _A_setup()

    def _B_setup():
        B, S, D, F = 4, 1, 512, 1792
        return {
            "x": _w((lambda *s: torch.randn(*s, dtype=torch.bfloat16) * 0.05)(B, S, D)),
            "w1": _w((lambda *s: torch.randn(*s, dtype=torch.bfloat16) * 0.1)(D, F)),
            "b1": _w((lambda *s: torch.randn(*s, dtype=torch.bfloat16) * 0.8)(F)),
            "w2": _w((lambda *s: torch.randn(*s, dtype=torch.bfloat16) * 0.1)(F, D)),
            "b2": _w((lambda *s: torch.randn(*s, dtype=torch.bfloat16) * 12.8)(D)),
        }
    _B_in = _B_setup()

    return {
        "A_x": _A_in["x"],
        "A_c_kv": _A_in["c_kv"],
        "A_w_q": _A_in["w_q"],
        "A_w_uk": _A_in["w_uk"],
        "A_w_uv": _A_in["w_uv"],
        "A_w_o": _A_in["w_o"],
        "B_x": _B_in["x"],
        "B_w1": _B_in["w1"],
        "B_b1": _B_in["b1"],
        "B_w2": _B_in["w2"],
        "B_b2": _B_in["b2"],
    }

def reference(A_x, A_c_kv, A_w_q, A_w_uk, A_w_uv, A_w_o, B_x, B_w1, B_b1, B_w2, B_b2):
    def _A_ref(x, c_kv, w_q, w_uk, w_uv, w_o):
        B, Sq, M = x.shape[0], x.shape[1], x.shape[2]
        Sk = c_kv.shape[1]
        H, D = w_q.shape[1], w_q.shape[2]
        q = torch.einsum("bqm,mhd->bhqd", x, w_q)
        k = torch.einsum("bkc,chd->bhkd", c_kv, w_uk)
        v = torch.einsum("bkc,chd->bhkd", c_kv, w_uv)
        scores = torch.einsum("bhqd,bhkd->bhqk", q, k) * (D ** -0.5)
        off = Sk - Sq
        qi = torch.arange(Sq)[:, None]
        kj = torch.arange(Sk)[None, :]
        cmask = kj <= (qi + off)
        scores = torch.where(cmask[None, None], scores, -1e9)
        p = _softmax(scores, axis=-1)
        ctx = torch.einsum("bhqk,bhkd->bhqd", p, v)
        return torch.einsum("bhqd,hdm->bqm", ctx, w_o)

    def _B_ref(x, w1, b1, w2, b2):
        h = torch.einsum("bsd,df->bsf", x, w1) + b1
        zero = torch.zeros_like(h)
        r = torch.where(h > zero, h, zero)
        h = r * r
        out = torch.einsum("bsf,fd->bsd", h, w2) + b2
        return out

    x = A_x
    xn = _layernorm(x, axis=-1)
    return x + _A_ref(xn, A_c_kv, A_w_q, A_w_uk, A_w_uv, A_w_o) + _B_ref(xn, B_w1, B_b1, B_w2, B_b2)
```

```cpp
#include <hip/hip_runtime.h>
#include <hip/hip_bf16.h>
#include <stdint.h>

typedef unsigned short u16;
__device__ __forceinline__ float bf2f(u16 x) { return __uint_as_float(((unsigned)x) << 16); }
__device__ __forceinline__ u16 f2bf(float f) { __hip_bfloat16 b = __float2bfloat16(f); return *reinterpret_cast<u16*>(&b); }

constexpr int B = 4, H = 32, SK = 4096, D = 128, M = 512, DC = 512, F = 1792;

__global__ void k_ln(const u16* x, float* xn) {
    int b = blockIdx.x, t = threadIdx.x;
    __shared__ float red[512];
    float v = bf2f(x[b * M + t]);
    red[t] = v; __syncthreads();
    for (int s = 256; s > 0; s >>= 1) { if (t < s) red[t] += red[t + s]; __syncthreads(); }
    float mean = red[0] / M; __syncthreads();
    float d = v - mean;
    red[t] = d * d; __syncthreads();
    for (int s = 256; s > 0; s >>= 1) { if (t < s) red[t] += red[t + s]; __syncthreads(); }
    float var = red[0] / M;
    xn[b * M + t] = d / sqrtf(var + 1e-5f);
}
__global__ void k_q(const float* xn, const u16* wq, float* q) {
    int col = blockIdx.x * 64 + threadIdx.x;
    float acc[B] = {0, 0, 0, 0};
    for (int m = 0; m < M; ++m) {
        float w = bf2f(wq[(size_t)m * (H * D) + col]);
        for (int b = 0; b < B; ++b) acc[b] += xn[b * M + m] * w;
    }
    for (int b = 0; b < B; ++b) q[b * H * D + col] = acc[b];
}
__global__ void k_qa(const float* q, const u16* wuk, float* qa) {
    int h = blockIdx.x, c = blockIdx.y * 64 + threadIdx.x;
    float acc[B] = {0, 0, 0, 0};
    for (int d = 0; d < D; ++d) {
        float w = bf2f(wuk[(size_t)c * (H * D) + h * D + d]);
        for (int b = 0; b < B; ++b) acc[b] += q[b * H * D + h * D + d] * w;
    }
    for (int b = 0; b < B; ++b) qa[(b * H + h) * DC + c] = acc[b] * 0.08838834764831845f;
}
__global__ void k_s(const float* qa, const u16* ckv, float* s) {
    int b = blockIdx.y, k = blockIdx.x * 64 + threadIdx.x;
    float acc[H];
    for (int h = 0; h < H; ++h) acc[h] = 0;
    for (int c = 0; c < DC; ++c) {
        float v = bf2f(ckv[((size_t)b * SK + k) * DC + c]);
        #pragma unroll
        for (int h = 0; h < H; ++h) acc[h] += qa[(b * H + h) * DC + c] * v;
    }
    for (int h = 0; h < H; ++h) s[((size_t)b * H + h) * SK + k] = acc[h];
}
__global__ void k_softmax(float* s) {
    int row = blockIdx.x, t = threadIdx.x;
    __shared__ float red[256];
    float* p = s + (size_t)row * SK;
    float mx = -INFINITY;
    for (int k = t; k < SK; k += 256) mx = fmaxf(mx, p[k]);
    red[t] = mx; __syncthreads();
    for (int st = 128; st > 0; st >>= 1) { if (t < st) red[t] = fmaxf(red[t], red[t + st]); __syncthreads(); }
    mx = red[0]; __syncthreads();
    float sum = 0;
    for (int k = t; k < SK; k += 256) { float e = expf(p[k] - mx); p[k] = e; sum += e; }
    red[t] = sum; __syncthreads();
    for (int st = 128; st > 0; st >>= 1) { if (t < st) red[t] += red[t + st]; __syncthreads(); }
    float inv = 1.f / red[0];
    for (int k = t; k < SK; k += 256) p[k] *= inv;
}
__global__ void k_pv(const float* p, const u16* ckv, float* ccp) {
    int b = blockIdx.y, sp = blockIdx.z, c = blockIdx.x * 64 + threadIdx.x;
    float acc[H];
    for (int h = 0; h < H; ++h) acc[h] = 0;
    for (int k = sp * 256; k < sp * 256 + 256; ++k) {
        float v = bf2f(ckv[((size_t)b * SK + k) * DC + c]);
        #pragma unroll
        for (int h = 0; h < H; ++h) acc[h] += p[((size_t)b * H + h) * SK + k] * v;
    }
    for (int h = 0; h < H; ++h) ccp[(((size_t)b * 16 + sp) * H + h) * DC + c] = acc[h];
}
__global__ void k_ctx(const float* ccp, const u16* wuv, float* ctx) {
    int h = blockIdx.x, b = blockIdx.y, d = threadIdx.x;
    __shared__ float cc[DC];
    for (int c = d; c < DC; c += 128) { float a = 0; for (int sp = 0; sp < 16; ++sp) a += ccp[(((size_t)b * 16 + sp) * H + h) * DC + c]; cc[c] = a; }
    __syncthreads();
    float acc = 0;
    for (int c = 0; c < DC; ++c) acc += cc[c] * bf2f(wuv[(size_t)c * (H * D) + h * D + d]);
    ctx[b * H * D + h * D + d] = acc;
}
__global__ void k_h(const float* xn, const u16* w1, const u16* b1, float* hid) {
    int f = blockIdx.x * 64 + threadIdx.x;
    float acc[B] = {0, 0, 0, 0};
    for (int m = 0; m < M; ++m) {
        float w = bf2f(w1[(size_t)m * F + f]);
        for (int b = 0; b < B; ++b) acc[b] += xn[b * M + m] * w;
    }
    for (int b = 0; b < B; ++b) { float v = acc[b] + bf2f(b1[f]); v = v > 0 ? v : 0; hid[b * F + f] = v * v; }
}
__global__ void k_out(const u16* x, const float* ctx, const u16* wo, const float* hid, const u16* w2, const u16* b2, u16* out) {
    int b = blockIdx.y, m = blockIdx.x * 64 + threadIdx.x;
    float a = 0;
    for (int i = 0; i < H * D; ++i) a += ctx[b * H * D + i] * bf2f(wo[(size_t)i * M + m]);
    float c = 0;
    for (int f = 0; f < F; ++f) c += hid[b * F + f] * bf2f(w2[(size_t)f * M + m]);
    c += bf2f(b2[m]);
    out[b * M + m] = f2bf(bf2f(x[b * M + m]) + a + c);
}

extern "C" void kernel_launch(void* const* d_in, const int* in_sizes, int n_in,
                              void* d_out, int out_size, void* d_ws, size_t ws_size,
                              hipStream_t stream) {
    const u16* x = (const u16*)d_in[0];
    const u16* ckv = (const u16*)d_in[1];
    const u16* wq = (const u16*)d_in[2];
    const u16* wuk = (const u16*)d_in[3];
    const u16* wuv = (const u16*)d_in[4];
    const u16* wo = (const u16*)d_in[5];
    const u16* w1 = (const u16*)d_in[7];
    const u16* b1 = (const u16*)d_in[8];
    const u16* w2 = (const u16*)d_in[9];
    const u16* b2 = (const u16*)d_in[10];
    float* ws = (float*)d_ws;
    float* xn = ws; ws += B * M;
    float* q = ws; ws += B * H * D;
    float* qa = ws; ws += B * H * DC;
    float* s = ws; ws += B * H * SK;
    float* ccp = ws; ws += B * 16 * H * DC;
    float* ctx = ws; ws += B * H * D;
    float* hid = ws; ws += B * F;
    k_ln<<<B, 512, 0, stream>>>(x, xn);
    k_q<<<64, 64, 0, stream>>>(xn, wq, q);
    k_qa<<<dim3(H, 8), 64, 0, stream>>>(q, wuk, qa);
    k_s<<<dim3(64, B), 64, 0, stream>>>(qa, ckv, s);
    k_softmax<<<B * H, 256, 0, stream>>>(s);
    k_pv<<<dim3(8, B, 16), 64, 0, stream>>>(s, ckv, ccp);
    k_ctx<<<dim3(H, B), 128, 0, stream>>>(ccp, wuv, ctx);
    k_h<<<F / 64, 64, 0, stream>>>(xn, w1, b1, hid);
    k_out<<<dim3(8, B), 64, 0, stream>>>(x, ctx, wo, hid, w2, b2, (u16*)d_out);
}
```

```cpp
#include <hip/hip_runtime.h>
#include <stdint.h>

namespace {

typedef unsigned short u16;
typedef unsigned long long u64;
typedef unsigned v4u __attribute__((ext_vector_type(4)));
typedef unsigned v2u __attribute__((ext_vector_type(2)));
typedef float f32x2 __attribute__((ext_vector_type(2)));
typedef float f32x4 __attribute__((ext_vector_type(4)));
typedef float f32x16 __attribute__((ext_vector_type(16)));
typedef short s16x4 __attribute__((ext_vector_type(4)));
typedef __bf16 bf16x2 __attribute__((ext_vector_type(2)));
typedef __bf16 bf16x8 __attribute__((ext_vector_type(8)));
#define GAS __attribute__((address_space(1)))
#define LAS __attribute__((address_space(3)))
#define DI __device__ __forceinline__
#define RLX_AGENT __ATOMIC_RELAXED, __HIP_MEMORY_SCOPE_AGENT
typedef GAS u64 gu64;
typedef __amdgpu_buffer_rsrc_t rsrc_t;

constexpr int SK = 4096, DM = 512, FF = 1792;
constexpr int NMLP = FF / 64;

constexpr unsigned WS_E = 0;
constexpr unsigned WS_H1 = 4096;
constexpr unsigned WS_HM = WS_H1 + 32 * 4 * 2048;
constexpr unsigned WS_H2 = WS_HM + 32 * 4 * 2048;
constexpr unsigned WS_H3 = WS_H2 + 4 * 65536;
constexpr unsigned WS_ST = WS_H3 + 32 * 4 * 32768;
constexpr unsigned WS_CK = WS_ST + 8 * 128 * 128;
constexpr unsigned WS_H5 = WS_CK + 128 * 1024;
constexpr unsigned WS_END = WS_H5 + 128 * 8192;
constexpr unsigned OOB = 0x7ffffff0u;

constexpr unsigned SENT = 0xffffffffu;
constexpr int AUX_SC1 = 16;
constexpr float LOG2E = 1.4426950408889634f;
constexpr unsigned SPIN_CAP = 1u << 17;

constexpr int TC = 160;
constexpr int T2 = 420;
constexpr int TAIL_DELAY = 440;
constexpr int CAP = 12;
constexpr int H5_SLEEP = 24;

constexpr unsigned L_SCR = 0, L_WUK = 98304;
constexpr unsigned L_WO = 0, L_W2 = 65536, L_WUV = 98304, L_W1 = 147456;
constexpr unsigned L_XN = 131072;
constexpr unsigned L_QS = L_XN + 4 * 1040;
constexpr unsigned L_CC = L_QS + 4 * 272;
constexpr unsigned L_CX = L_CC + 4 * 272;
constexpr unsigned L_HS = L_CX + 4 * 272;
constexpr unsigned L_EP = L_HS + 4 * 144;
constexpr unsigned L_WT = L_EP + 16;
constexpr unsigned L_FL = L_WT + 512;
static_assert(L_FL + 32 <= L_W1 && (L_FL & 15) == 0 && (L_WT & 15) == 0, "G1 LDS map");
constexpr unsigned L_C = 0, L_QA = 131072;
constexpr unsigned L_P = L_QA, L_MX = L_QA + 8192, L_LS = L_MX + 512;
constexpr unsigned L_CK = L_QA + 12288;
constexpr unsigned LDS_BYTES = 163840;

struct Params {
    const u16 *x, *ckv, *wq, *wuk, *wuv, *wo, *w1, *b1, *w2, *b2;
    u16* out;
    unsigned char* ws;
};

DI float bf2f(unsigned v) { return __uint_as_float(v << 16); }
DI float bflo(unsigned v) { return __uint_as_float(v << 16); }
DI float bfhi(unsigned v) { return __uint_as_float(v & 0xffff0000u); }
DI unsigned pk2(float lo, float hi) { f32x2 a = {lo, hi}; return __builtin_bit_cast(unsigned, __builtin_convertvector(a, bf16x2)); }
DI float rbf(float v) { return bflo(pk2(v, v) & 0xffffu); }
DI rsrc_t mk_rsrc(const void* p, unsigned bytes) { return __builtin_amdgcn_make_buffer_rsrc((void*)p, (short)0, (int)bytes, 0x00020000); }
DI u64 rt() { return __builtin_amdgcn_s_memrealtime(); }
DI void wait_until(u64 t) { for (int i = 0; i < 4096 && rt() < t; ++i) __builtin_amdgcn_s_sleep(1); }

DI void dma16(rsrc_t rs, unsigned voff, unsigned soff, unsigned lds) {
    unsigned keep;
    asm volatile("s_nop 4\n\ts_mov_b32 %0, m0\n\ts_mov_b32 m0, %1\n\ts_nop 0\n\tbuffer_load_dwordx4 %2, %3, %4 offen nt lds\n\ts_mov_b32 m0, %0"
                 : "=&s"(keep) : "s"(lds), "v"(voff), "s"(rs), "s"(soff) : "memory");
}
DI void dma16k(rsrc_t rs, unsigned voff, unsigned soff, unsigned lds) {
    unsigned keep;
    asm volatile("s_nop 4\n\ts_mov_b32 %0, m0\n\ts_mov_b32 m0, %1\n\ts_nop 0\n\tbuffer_load_dwordx4 %2, %3, %4 offen lds\n\ts_mov_b32 m0, %0"
                 : "=&s"(keep) : "s"(lds), "v"(voff), "s"(rs), "s"(soff) : "memory");
}
template <int N> DI void wait_vm() { asm volatile("s_waitcnt vmcnt(%0)" ::"n"(N) : "memory"); }
DI void bar() { asm volatile("s_waitcnt lgkmcnt(0)\n\ts_barrier" ::: "memory"); }

DI v4u ld_sc1(rsrc_t rs, unsigned off) { return __builtin_amdgcn_raw_buffer_load_b128(rs, off, 0, AUX_SC1); }
DI v2u ld8_sc1(rsrc_t rs, unsigned off) { return __builtin_amdgcn_raw_buffer_load_b64(rs, off, 0, AUX_SC1); }
DI unsigned ld4_sc1(rsrc_t rs, unsigned off) { return __builtin_amdgcn_raw_buffer_load_b32(rs, off, 0, AUX_SC1); }
DI void st_sc1(rsrc_t rs, unsigned off, v4u v) { __builtin_amdgcn_raw_buffer_store_b128(v, rs, off, 0, AUX_SC1); }

DI f32x4 mfma16(v4u a, v4u b, f32x4 c) { return __builtin_amdgcn_mfma_f32_16x16x32_bf16(__builtin_bit_cast(bf16x8, a), __builtin_bit_cast(bf16x8, b), c, 0, 0, 0); }
DI f32x16 mfma32(v4u a, v4u b, f32x16 c) { return __builtin_amdgcn_mfma_f32_32x32x16_bf16(__builtin_bit_cast(bf16x8, a), __builtin_bit_cast(bf16x8, b), c, 0, 0, 0); }

DI v4u lds128(LAS unsigned char* L, unsigned off) { return *(LAS v4u*)(L + off); }
DI v2u tr64(LAS unsigned char* L, unsigned off) { return __builtin_bit_cast(v2u, __builtin_amdgcn_ds_read_tr16_b64_v4i16((LAS s16x4*)(L + off))); }
DI v4u tr_frag(LAS unsigned char* L, unsigned off0, unsigned off1) { v2u a = tr64(L, off0), b = tr64(L, off1); return v4u{a.x, a.y, b.x, b.y}; }

template <int CTRL> DI float dpp(float x) { return __builtin_bit_cast(float, __builtin_amdgcn_mov_dpp(__builtin_bit_cast(int, x), CTRL, 0xf, 0xf, true)); }
constexpr int XOR1 = 0xB1, XOR2 = 0x4E, HMIR = 0x141, MIR = 0x140, ROR4 = 0x124, ROR8 = 0x128;
DI float sum16x(float x) { auto s = __builtin_amdgcn_permlane16_swap(__float_as_uint(x), __float_as_uint(x), false, false); return __uint_as_float(s[0]) + __uint_as_float(s[1]); }
DI float sum32x(float x) { auto s = __builtin_amdgcn_permlane32_swap(__float_as_uint(x), __float_as_uint(x), false, false); return __uint_as_float(s[0]) + __uint_as_float(s[1]); }
DI float fmx(float a, float b) { return __builtin_amdgcn_fmed3f(a, b, __builtin_inff()); }
DI float max16x(float x) { auto s = __builtin_amdgcn_permlane16_swap(__float_as_uint(x), __float_as_uint(x), false, false); return fmx(__uint_as_float(s[0]), __uint_as_float(s[1])); }
DI float max32x(float x) { auto s = __builtin_amdgcn_permlane32_swap(__float_as_uint(x), __float_as_uint(x), false, false); return fmx(__uint_as_float(s[0]), __uint_as_float(s[1])); }
DI float wave_sum(float x) {
    x += dpp<XOR1>(x); x += dpp<XOR2>(x); x += dpp<HMIR>(x); x += dpp<MIR>(x);
    return sum32x(sum16x(x));
}
DI float red32(float a, float b) { auto s = __builtin_amdgcn_permlane32_swap(__float_as_uint(a), __float_as_uint(b), false, false); return __uint_as_float(s[0]) + __uint_as_float(s[1]); }
DI float red16(float a, float b) { auto s = __builtin_amdgcn_permlane16_swap(__float_as_uint(a), __float_as_uint(b), false, false); return __uint_as_float(s[0]) + __uint_as_float(s[1]); }
DI unsigned max4u(v4u v) { return max(max(v.x, v.y), max(v.z, v.w)); }

DI u64 next_epoch(u64 e) { e += 1; return (unsigned)e == SENT ? e + 1 : e; }
DI v2u gran(float v, unsigned t8, unsigned t32) { return v2u{(__float_as_uint(v) & 0xffffff00u) | t8, t32}; }
DI unsigned gran_bad(v2u g, unsigned t8, unsigned t32) { return ((g.x ^ t8) & 0xffu) | (g.y ^ t32); }
DI float gran_val(v2u g) { return __uint_as_float(g.x & 0xffffff00u); }

DI v4u ln_row(v4u xv) {
    const float v[8] = {bflo(xv.x), bfhi(xv.x), bflo(xv.y), bfhi(xv.y), bflo(xv.z), bfhi(xv.z), bflo(xv.w), bfhi(xv.w)};
    float s = 0.f;
#pragma unroll
    for (int i = 0; i < 8; ++i) s += v[i];
    const float mean = wave_sum(s) * (1.f / DM);
    float q2 = 0.f;
#pragma unroll
    for (int i = 0; i < 8; ++i) { const float d = v[i] - mean; q2 += d * d; }
    const float var = rbf(wave_sum(q2) * (1.f / DM));
    const float mb = rbf(mean);
    const float den = rbf(sqrtf(rbf(var + 1e-5f)));
    float o[8];
#pragma unroll
    for (int i = 0; i < 8; ++i) o[i] = rbf(v[i] - mb) / den;
    return v4u{pk2(o[0], o[1]), pk2(o[2], o[3]), pk2(o[4], o[5]), pk2(o[6], o[7])};
}
DI unsigned ck_key(unsigned tag, unsigned i) {
    unsigned x = (tag + 0x632BE5ABu) * (i == 0 ? 0x9E3779B1u : i == 1 ? 0x85EBCA77u : i == 2 ? 0xC2B2AE3Du : 0x27D4EB2Fu);
    x ^= x >> 15;
    return (x & ~7u) | (i << 1) | (i == 3 ? 1u : 0u);
}
DI unsigned xor8w(unsigned xx) {
    int x = (int)xx;
    x ^= __builtin_amdgcn_mov_dpp(x, XOR1, 0xf, 0xf, true);
    x ^= __builtin_amdgcn_mov_dpp(x, XOR2, 0xf, 0xf, true);
    x ^= __builtin_amdgcn_mov_dpp(x, HMIR, 0xf, 0xf, true);
    return (unsigned)x;
}
DI unsigned xor8(v4u v) {
    int x = (int)(v.x ^ v.y ^ v.z ^ v.w);
    x ^= __builtin_amdgcn_mov_dpp(x, XOR1, 0xf, 0xf, true);
    x ^= __builtin_amdgcn_mov_dpp(x, XOR2, 0xf, 0xf, true);
    x ^= __builtin_amdgcn_mov_dpp(x, HMIR, 0xf, 0xf, true);
    return (unsigned)x;
}

DI void role_g1(const Params& p, LAS unsigned char* L, const int xc, const int k, const int w, const int lane) {
    const u64 t_start = rt();
    const int tid = threadIdx.x;
    const int h = xc * 4 + (k >> 2), j = k & 3;
    const bool has_mlp = h < NMLP;
    const rsrc_t rws = mk_rsrc(p.ws, WS_END);
    const unsigned ldsb = (unsigned)(uintptr_t)L;
    const int g = lane >> 4, r16 = lane & 15, tq = r16 >> 2, tp = lane & 3;
    const unsigned hj = (unsigned)(h * 4 + j);
    const unsigned o2 = WS_H2 + r16 * 65536 + h * 2048 + (32 * j + 4 * w + g) * 16;
    unsigned braw = 0;
    v2u xfin = {0u, 0u}, bfin = {0u, 0u};

    if (w == 4 || w == 5) {
        const int lw = w - 4;
        const rsrc_t rq = mk_rsrc(p.wq, DM * 4096 * 2);
        const unsigned f = ((lane >> 4) & 1) | (lw << 1);
        const unsigned vq = (unsigned)(lane >> 3) * 8192u + 16u * ((lane & 7) ^ (f << 1));
        const unsigned sq = (unsigned)(256 * (j >> 1) + 8 * lw) * 8192u + h * 256 + (j & 1) * 128;
#pragma unroll
        for (int ii = 0; ii < 16; ++ii) dma16(rq, vq, sq + ii * 16 * 8192u, ldsb + L_SCR + (2 * ii + lw) * 1024);
    }
    v4u xv0 = {0u, 0u, 0u, 0u};
    u64 e0 = 0;
    if (w < 4) {
        xv0 = *(const GAS v4u*)(p.x + w * DM + lane * 8);
        if (w == 0) { e0 = __hip_atomic_load((gu64*)(p.ws + WS_E), RLX_AGENT); if (lane < 8) *(LAS unsigned*)(L + L_FL + lane * 4) = 0u; }
    }
    bar();
    if (w >= 6) {
        const int lw = w - 6;
        const rsrc_t ruk = mk_rsrc(p.wuk, DM * 4096 * 2);
        const unsigned vo0 = (unsigned)g * 8192u + 16u * (r16 ^ (4 * lw + g)), vo1 = (unsigned)g * 8192u + 16u * (r16 ^ (8 + 4 * lw + g));
        const unsigned sb = (unsigned)(128 * j + 4 * lw) * 8192u + h * 256;
#pragma unroll
        for (int ii = 0; ii < 16; ++ii) dma16(ruk, (ii & 1) ? vo1 : vo0, sb + ii * 8 * 8192u, ldsb + L_WUK + (2 * ii + lw) * 1024);
    } else if (w >= 4) {
        wait_vm<0>();
        if (lane == 0) *(LAS unsigned*)(L + L_FL + w * 4) = 1u;
    } else {
        *(LAS v4u*)(L + L_XN + w * 1040 + lane * 16) = ln_row(xv0);
        if (w == 0 && lane == 0) *(LAS u64*)(L + L_EP) = next_epoch(e0);
        asm volatile("" ::: "memory");
        if (lane == 0) *(LAS unsigned*)(L + L_FL + w * 4) = 1u;
        const unsigned f0 = ((tq >> 1) & 1) | ((g & 1) << 1);
        unsigned ao = L_SCR + (8 * g + tq) * 128 + 16 * ((2 * w + (tp >> 1)) ^ (f0 << 1)) + 8 * (tp & 1);
        unsigned bo = L_XN + (r16 & 3) * 1040 + (j >> 1) * 512 + g * 16;
        unsigned o1 = WS_H1 + hj * 2048 + w * 512 + r16 * 64 + g * 16;
        asm volatile("" : "+v"(ao), "+v"(bo), "+v"(o1));
        for (unsigned spins = 0;; ++spins) {
            asm volatile("" ::: "memory");
            const v4u f0_ = *(volatile LAS v4u*)(L + L_FL);
            const v2u f1_ = *(volatile LAS v2u*)(L + L_FL + 16);
            if (__builtin_amdgcn_readfirstlane(f0_.x & f0_.y & f0_.z & f0_.w & f1_.x & f1_.y) || spins > SPIN_CAP) break;
        }
        asm volatile("" ::: "memory");
        const u64 e1 = *(LAS u64*)(L + L_EP);
        const unsigned t32 = (unsigned)e1, t8 = (unsigned)(e1 >> 32) & 0xffu;
        f32x4 acc = {0.f, 0.f, 0.f, 0.f};
        v4u af[8], bf[8];
#pragma unroll
        for (int ks = 0; ks < 4; ++ks) { af[ks] = tr_frag(L, ao + ks * 4096, ao + ks * 4096 + 512); bf[ks] = lds128(L, bo + ks * 64); __builtin_amdgcn_sched_barrier(0); }
#pragma unroll
        for (int ks = 0; ks < 8; ++ks) {
            if (ks + 4 < 8) { af[ks + 4] = tr_frag(L, ao + (ks + 4) * 4096, ao + (ks + 4) * 4096 + 512); bf[ks + 4] = lds128(L, bo + (ks + 4) * 64); }
            __builtin_amdgcn_sched_barrier(0);
            acc = mfma16(af[ks], bf[ks], acc);
            __builtin_amdgcn_sched_barrier(0);
        }
        if (r16 < 4) {
            const v2u g0 = gran(acc[0], t8, t32), g1 = gran(acc[1], t8, t32), g2_ = gran(acc[2], t8, t32), g3 = gran(acc[3], t8, t32);
            st_sc1(rws, o1, v4u{g0.x, g0.y, g1.x, g1.y});
            st_sc1(rws, o1 + 256, v4u{g2_.x, g2_.y, g3.x, g3.y});
        }
    }
    wait_vm<0>();
    bar();
    const u64 ep = *(LAS u64*)(L + L_EP);
    const unsigned tag32 = (unsigned)ep, tag8 = (unsigned)(ep >> 32) & 0xffu;
    v4u auk[4];
#pragma unroll
    for (int s = 0; s < 4; ++s) auk[s] = lds128(L, L_WUK + (16 * w + r16) * 256 + 16 * ((4 * s + g) ^ r16));
    unsigned bqs = L_QS + (r16 & 3) * 272 + g * 16;
    asm volatile("" : "+v"(bqs));
    {
        const int b = tid >> 7, d = tid & 127;
        const unsigned o = WS_H1 + (unsigned)(h * 4 + (d >> 6)) * 2048 + ((d >> 4) & 3) * 512 + ((d >> 1) & 1) * 256 + b * 64 + ((d >> 2) & 3) * 16 + (d & 1) * 8;
        v2u a0, a1;
        for (unsigned spins = 0;; ++spins) {
            asm volatile("" ::: "memory");
            a0 = ld8_sc1(rws, o);
            a1 = ld8_sc1(rws, o + 2 * 2048);
            if (__all((gran_bad(a0, tag8, tag32) | gran_bad(a1, tag8, tag32)) == 0) || spins > SPIN_CAP) break;
            __builtin_amdgcn_s_sleep(2);
        }
        const float q = gran_val(a0) + gran_val(a1);
        *(LAS u16*)(L + L_QS + b * 272 + d * 2) = (u16)pk2(q, q);
    }
    bar();
    {
        f32x4 acc = {0.f, 0.f, 0.f, 0.f};
        v4u qv[4];
#pragma unroll
        for (int s = 0; s < 4; ++s) qv[s] = lds128(L, bqs + s * 64);
        __builtin_amdgcn_sched_barrier(0);
#pragma unroll
        for (int s = 0; s < 4; ++s) acc = mfma16(auk[s], qv[s], acc);
        if (r16 < 4) st_sc1(rws, o2, v4u{pk2(acc[0], acc[1]), tag32, pk2(acc[2], acc[3]), tag32});
    }
    if (w == 0) {
        const int m0 = 128 * j + 16 * (h & 7) + 4 * (lane & 3);
        xfin = *(const GAS v2u*)(p.x + (h >> 3) * DM + m0);
        bfin = *(const GAS v2u*)(p.b2 + m0);
    }
    if (has_mlp && w >= 4) braw = p.b1[h * 64 + lane];
    unsigned t3 = 0;
    if (w == 7) {
        const unsigned char* a = (const unsigned char*)p.wuv + (128 * j) * 8192u + h * 256;
        if (lane == 1) a = (const unsigned char*)p.wuv + (128 * j + 127) * 8192u + h * 256;
        if (lane == 2) a = (const unsigned char*)p.wo + h * 131072u;
        if (lane == 3) a = (const unsigned char*)p.w1 + (128 * j) * 3584u;
        if (lane == 4) a = (const unsigned char*)p.w2 + (has_mlp ? h : 0) * 65536u;
        if (lane < 5) t3 = __builtin_nontemporal_load((const GAS unsigned*)a);
    }
    wait_until(t_start + TAIL_DELAY);
    const rsrc_t ro = mk_rsrc(p.wo, 4096u * DM * 2u);
    const unsigned vo0 = 16u * (lane ^ ((w & 3) << 1)), vo1 = 16u * (lane ^ (((w & 3) | 4) << 1));
    {
        const unsigned f3 = (unsigned)g | (((w >> 1) & 1) << 2);
        if (has_mlp) {
            const rsrc_t r1 = mk_rsrc(p.w1, DM * FF * 2);
            const unsigned f = ((lane >> 4) & 1) | ((w & 1) << 1);
            const unsigned v1 = (unsigned)(lane >> 3) * 3584u + 16u * ((lane & 7) ^ (f << 1));
            const unsigned s1 = (unsigned)(128 * j + 8 * w) * 3584u + h * 128;
#pragma unroll
            for (int ii = 0; ii < 2; ++ii) dma16(r1, v1, s1 + ii * 64 * 3584u, ldsb + L_W1 + (8 * ii + w) * 1024);
        }
        {
            const unsigned so = (unsigned)(128 * h + 32 * j + w) * 1024u;
#pragma unroll
            for (int i = 0; i < 4; ++i) dma16k(ro, (i & 1) ? vo1 : vo0, so + i * 8192u, ldsb + L_WO + (8 * i + w) * 1024);
        }
        wait_vm<4>();
        bar();
        if (has_mlp && w >= 4) {
            const int lw = w - 4;
            f32x4 acc = {0.f, 0.f, 0.f, 0.f};
            const unsigned f0 = ((tq >> 1) & 1) | ((g & 1) << 1);
            const unsigned a1 = L_W1 + (8 * g + tq) * 128 + 16 * ((2 * lw + (tp >> 1)) ^ (f0 << 1)) + 8 * (tp & 1);
            const unsigned bo = L_XN + (r16 & 3) * 1040 + j * 256 + g * 16;
#pragma unroll
            for (int ks = 0; ks < 4; ++ks) acc = mfma16(tr_frag(L, a1 + ks * 4096, a1 + ks * 4096 + 512), lds128(L, bo + ks * 64), acc);
            if (r16 < 4) {
                const unsigned o = WS_HM + hj * 2048 + lw * 512 + r16 * 64 + g * 16;
                const v2u g0 = gran(acc[0], tag8, tag32), g1 = gran(acc[1], tag8, tag32), g2 = gran(acc[2], tag8, tag32), g3 = gran(acc[3], tag8, tag32);
                st_sc1(rws, o, v4u{g0.x, g0.y, g1.x, g1.y});
                st_sc1(rws, o + 256, v4u{g2.x, g2.y, g3.x, g3.y});
            }
        }
        const rsrc_t ruv = mk_rsrc(p.wuv, DM * 4096 * 2);
        const unsigned voff = (unsigned)g * 8192u + 16u * (r16 ^ (f3 << 1));
        const unsigned sb = (unsigned)(128 * j + 4 * w) * 8192u + h * 256;
#pragma unroll
        for (int ii = 0; ii < 4; ++ii) dma16(ruv, voff, sb + ii * 32 * 8192u, ldsb + L_WUV + (8 * ii + w) * 1024);
        if (has_mlp) {
            const rsrc_t r2 = mk_rsrc(p.w2, FF * DM * 2);
            const unsigned v2 = (unsigned)g * 1024u + 16u * (r16 ^ (f3 << 1));
            const unsigned s2 = (unsigned)(64 * h + 4 * w) * 1024u + j * 256;
#pragma unroll
            for (int ii = 0; ii < 2; ++ii) dma16(r2, v2, s2 + ii * 32 * 1024u, ldsb + L_W2 + (8 * ii + w) * 1024);
        }
        {
            const unsigned so = (unsigned)(128 * h + 32 * ((j + 1) & 3) + w) * 1024u;
#pragma unroll
            for (int i = 0; i < 4; ++i) dma16k(ro, (i & 1) ? vo1 : vo0, so + i * 8192u, ldsb + L_WO + 32768 + (8 * i + w) * 1024);
        }
        wait_vm<0>();
        asm volatile("" :: "v"(t3));
        asm volatile("" :: "v"(xfin), "v"(bfin), "v"(braw));
    }
    bar();
    const unsigned fsw = (tq | ((g & 1) << 2)) << 1;
    const unsigned tro = (8 * g + tq) * 256 + 16 * ((2 * w + (tp >> 1)) ^ fsw) + 8 * (tp & 1);
    v4u auv[4], ao[4][4];
    unsigned tw[4];
#pragma unroll
    for (int t = 0; t < 4; ++t) tw[t] = L_WO + (8 * g + tq) * 1024 + (w >> 1) * 256 + 16 * ((8 * (w & 1) + 2 * t + (tp >> 1)) ^ fsw) + 8 * (tp & 1);
#pragma unroll
    for (int r = 0; r < 2; ++r)
#pragma unroll
        for (int t = 0; t < 4; ++t) ao[r][t] = tr_frag(L, r * 32768 + tw[t], r * 32768 + tw[t] + 4096);
    bar();
#pragma unroll
    for (int r = 2; r < 4; ++r) {
        const unsigned so = (unsigned)(128 * h + 32 * ((j + r) & 3) + w) * 1024u;
#pragma unroll
        for (int i = 0; i < 4; ++i) dma16k(ro, (i & 1) ? vo1 : vo0, so + i * 8192u, ldsb + L_WO + (r - 2) * 32768 + (8 * i + w) * 1024);
    }
#pragma unroll
    for (int ks = 0; ks < 4; ++ks) auv[ks] = tr_frag(L, L_WUV + tro + ks * 8192, L_WUV + tro + ks * 8192 + 1024);
    if (has_mlp && w >= 4) {
        const int b = w - 4, f = lane;
        const unsigned o = WS_HM + (unsigned)(h * 4) * 2048 + (f >> 4) * 512 + ((f >> 1) & 1) * 256 + b * 64 + ((f >> 2) & 3) * 16 + (f & 1) * 8;
        v2u a[4];
        for (unsigned spins = 0;; ++spins) {
            asm volatile("" ::: "memory");
            unsigned bad = 0;
#pragma unroll
            for (int i = 0; i < 4; ++i) { a[i] = ld8_sc1(rws, o + i * 2048); }
#pragma unroll
            for (int i = 0; i < 4; ++i) bad |= gran_bad(a[i], tag8, tag32);
            if (__all(bad == 0) || spins > SPIN_CAP) break;
            __builtin_amdgcn_s_sleep(4);
        }
        float hv = rbf((gran_val(a[0]) + gran_val(a[1])) + (gran_val(a[2]) + gran_val(a[3])));
        hv = rbf(hv + bf2f(braw));
        hv = hv > 0.f ? hv : 0.f;
        hv = hv * hv;
        *(LAS u16*)(L + L_HS + b * 144 + f * 2) = (u16)pk2(hv, hv);
    }
    wait_vm<0>();
    bar();
#pragma unroll
    for (int r = 2; r < 4; ++r)
#pragma unroll
        for (int t = 0; t < 4; ++t) ao[r][t] = tr_frag(L, (r - 2) * 32768 + tw[t], (r - 2) * 32768 + tw[t] + 4096);
    f32x4 oacc[4];
#pragma unroll
    for (int t = 0; t < 4; ++t) oacc[t] = f32x4{0.f, 0.f, 0.f, 0.f};
    if (has_mlp && (w >> 1) == j) {
        const unsigned bo = L_HS + (r16 & 3) * 144 + g * 16;
        const v4u h0 = lds128(L, bo), h1 = lds128(L, bo + 64);
#pragma unroll
        for (int t = 0; t < 4; ++t) {
            const unsigned a2 = L_W2 + (8 * g + tq) * 256 + 16 * ((8 * (w & 1) + 2 * t + (tp >> 1)) ^ fsw) + 8 * (tp & 1);
            oacc[t] = mfma16(tr_frag(L, a2, a2 + 1024), h0, oacc[t]);
            oacc[t] = mfma16(tr_frag(L, a2 + 8192, a2 + 8192 + 1024), h1, oacc[t]);
        }
    }
    unsigned bcc = L_CC + (r16 & 3) * 272 + g * 16, wcx = L_CX + r16 * 272 + (16 * w + 4 * g) * 2, o5 = WS_H5 + hj * 8192 + w * 1024 + r16 * 64 + g * 16, bcx[4];
#pragma unroll
    for (int r = 0; r < 4; ++r) { bcx[r] = L_CX + (r16 & 3) * 272 + g * 16 + ((j + r) & 3) * 64; asm volatile("" : "+v"(bcx[r])); }
    asm volatile("" : "+v"(bcc), "+v"(wcx), "+v"(o5));
    if (w < 2) {
        const unsigned so = WS_ST + xc * 16384 + tid * 128 + (h & 3) * 16;
        v4u st;
        for (unsigned spins = 0;; ++spins) {
            asm volatile("" ::: "memory");
            st = ld_sc1(rws, so);
            if (__all(((st.y ^ tag32) | (st.w ^ tag32)) == 0) || spins > SPIN_CAP) break;
            __builtin_amdgcn_s_sleep(2);
        }
        const float m0 = __uint_as_float(st.x);
        float mx = fmaxf(m0, dpp<XOR1>(m0)); mx = fmaxf(mx, dpp<XOR2>(mx)); mx = fmaxf(mx, dpp<HMIR>(mx)); mx = fmaxf(mx, dpp<MIR>(mx)); mx = max16x(mx);
        const float e = __builtin_amdgcn_exp2f((m0 - mx) * LOG2E);
        float es = e * __uint_as_float(st.z); es += dpp<XOR1>(es); es += dpp<XOR2>(es); es += dpp<HMIR>(es); es += dpp<MIR>(es); es = sum16x(es);
        *(LAS float*)(L + L_WT + tid * 4) = e / es;
    }
    bar();
    {
        const int b = w >> 1, cg = lane >> 3, oct = (w & 1) * 8 + (lane & 7);
        const unsigned po = WS_H3 + hj * 32768 + (b * 32 + cg * 4) * 256 + oct * 16;
        const unsigned co = WS_CK + (b * 32 + cg * 4) * 1024 + (j * 2 + (w & 1)) * 128 + (h & 7) * 16 + (h >> 3) * 4;
        const f32x4 e = *(LAS f32x4*)(L + L_WT + (b * 32 + cg * 4) * 4);
        v4u pc[4];
        unsigned ck[4];
        const unsigned k0 = ck_key(tag32, 0), k1 = ck_key(tag32, 1), k2 = ck_key(tag32, 2), k3 = ck_key(tag32, 3);
#pragma unroll
        for (int i = 0; i < 4; ++i) pc[i] = ld_sc1(rws, po + i * 256);
#pragma unroll
        for (int i = 0; i < 4; ++i) ck[i] = ld4_sc1(rws, co + i * 1024);
        f32x2 a2[4];
#define COMBINE() do { _Pragma("unroll") for (int q = 0; q < 4; ++q) a2[q] = e[0] * f32x2{bflo(pc[0][q]), bfhi(pc[0][q])}; \
            _Pragma("unroll") for (int i = 1; i < 4; ++i) _Pragma("unroll") for (int q = 0; q < 4; ++q) a2[q] += e[i] * f32x2{bflo(pc[i][q]), bfhi(pc[i][q])}; } while (0)
        COMBINE();
        {
            const v4u xs = (pc[0] ^ pc[1]) ^ (pc[2] ^ pc[3]);
            const unsigned got = xor8w((xs.x ^ xs.y) ^ (xs.z ^ xs.w)) ^ (k0 ^ k1 ^ k2 ^ k3);
            if (__builtin_amdgcn_ballot_w64(got != ((ck[0] ^ ck[1]) ^ (ck[2] ^ ck[3]))) != 0) {
                for (unsigned spins = 0; spins <= SPIN_CAP; ++spins) {
                    asm volatile("" ::: "memory");
                    bool nd[4];
                    nd[0] = (xor8(pc[0]) ^ k0) != ck[0]; nd[1] = (xor8(pc[1]) ^ k1) != ck[1]; nd[2] = (xor8(pc[2]) ^ k2) != ck[2]; nd[3] = (xor8(pc[3]) ^ k3) != ck[3];
                    if (__all(!nd[0] && !nd[1] && !nd[2] && !nd[3])) break;
                    v4u n[4];
                    unsigned c[4];
#pragma unroll
                    for (int i = 0; i < 4; ++i) { n[i] = ld_sc1(rws, nd[i] ? po + i * 256 : OOB); c[i] = ld4_sc1(rws, nd[i] ? co + i * 1024 : OOB); }
#pragma unroll
                    for (int i = 0; i < 4; ++i) if (nd[i]) { pc[i] = n[i]; ck[i] = c[i]; }
                    if (spins > 2) __builtin_amdgcn_s_sleep(2);
                }
                COMBINE();
            }
        }
        const float a[8] = {a2[0].x, a2[0].y, a2[1].x, a2[1].y, a2[2].x, a2[2].y, a2[3].x, a2[3].y};
        const float b0 = red32(a[0], a[4]), b1 = red32(a[1], a[5]), b2 = red32(a[2], a[6]), b3 = red32(a[3], a[7]);
        const float c0 = red16(b0, b2), c1 = red16(b1, b3);
        const bool hi8 = (lane & 8) != 0;
        const float keep = hi8 ? c1 : c0, send = hi8 ? c0 : c1;
        const float cc = keep + dpp<ROR8>(send);
        const int el = ((lane >> 5) << 2) | (((lane >> 4) & 1) << 1) | ((lane >> 3) & 1);
        *(LAS u16*)(L + L_CC + b * 272 + (oct * 8 + el) * 2) = (u16)pk2(cc, cc);
    }
    bar();
    {
        f32x4 acc = {0.f, 0.f, 0.f, 0.f};
        v4u cf[4];
#pragma unroll
        for (int ks = 0; ks < 4; ++ks) cf[ks] = lds128(L, bcc + ks * 64);
        __builtin_amdgcn_sched_barrier(0);
#pragma unroll
        for (int ks = 0; ks < 4; ++ks) acc = mfma16(auv[ks], cf[ks], acc);
        if (r16 < 4) *(LAS v2u*)(L + wcx) = v2u{pk2(acc[0], acc[1]), pk2(acc[2], acc[3])};
    }
    bar();
    {
        v4u cx[4];
#pragma unroll
        for (int r = 0; r < 4; ++r) cx[r] = lds128(L, bcx[r]);
        __builtin_amdgcn_sched_barrier(0);
#pragma unroll
        for (int r = 0; r < 4; ++r) {
#pragma unroll
            for (int t = 0; t < 4; ++t) oacc[t] = mfma16(ao[r][t], cx[r], oacc[t]);
        }
        if (r16 < 4) {
#pragma unroll
            for (int t = 0; t < 4; ++t) st_sc1(rws, o5 + t * 256, __builtin_bit_cast(v4u, oacc[t]));
        }
    }
    if (w == 0) {
        const int b = h >> 3, p4 = lane >> 2, qd = lane & 3;
        const int m0 = 128 * j + 16 * (h & 7) + 4 * qd;
        const v2u xv = xfin, bv = bfin;
        const unsigned o = WS_H5 + p4 * 8192 + (8 * j + (h & 7)) * 256 + b * 64 + qd * 16;
        __builtin_amdgcn_s_sleep(H5_SLEEP);
        v4u v[8];
        for (unsigned spins = 0;; ++spins) {
            asm volatile("" ::: "memory");
#pragma unroll
            for (int i = 0; i < 8; ++i) v[i] = ld_sc1(rws, o + i * 16 * 8192);
            unsigned mxu = 0;
#pragma unroll
            for (int i = 0; i < 8; ++i) mxu = max(mxu, max4u(v[i]));
            if (__all(mxu != SENT) || spins > SPIN_CAP) break;
        }
        float A[4];
#pragma unroll
        for (int i = 0; i < 4; ++i)
            A[i] = ((__uint_as_float(v[0][i]) + __uint_as_float(v[1][i])) + (__uint_as_float(v[2][i]) + __uint_as_float(v[3][i]))) +
                   ((__uint_as_float(v[4][i]) + __uint_as_float(v[5][i])) + (__uint_as_float(v[6][i]) + __uint_as_float(v[7][i])));
        float c0 = sum16x(red32(A[0], A[2])), c1 = sum16x(red32(A[1], A[3]));
        c0 += dpp<ROR8>(c0); c1 += dpp<ROR8>(c1);
        c0 += dpp<ROR4>(c0); c1 += dpp<ROR4>(c1);
        const bool hi = lane >= 32;
        const unsigned xw = hi ? xv.y : xv.x, bw = hi ? bv.y : bv.x;
        if ((lane & 28) == 0) *(GAS unsigned*)(p.out + b * DM + m0 + 2 * (lane >> 5)) = pk2(c0 + (bflo(xw) + bflo(bw)), c1 + (bfhi(xw) + bfhi(bw)));
        if (blockIdx.x == 0 && lane == 0) __hip_atomic_store((gu64*)(p.ws + WS_E), ep, RLX_AGENT);
    }
}

DI void role_g2(const Params& p, LAS unsigned char* L, const int xc, const int k, const int w, const int lane) {
    const u64 t_start = rt();
    const int g2 = xc * 16 + (k - 16), b = g2 >> 5, chunk = g2 & 31;
    const rsrc_t rws = mk_rsrc(p.ws, WS_END);
    const unsigned ldsb = (unsigned)(uintptr_t)L;
    const int g = lane >> 4, r16 = lane & 15, tq = r16 >> 2, tp = lane & 3;
    const int kq = w >> 1, hh = w & 1;
    unsigned tag32 = 0;

    if (w < 4) {
        const rsrc_t rc = mk_rsrc(p.ckv, 4u * SK * 1024u);
        const unsigned sc_ = (unsigned)(g2 * 128 + w) * 1024u;
        unsigned vo[4];
#pragma unroll
        for (int i = 0; i < 4; ++i) vo[i] = 16u * (lane ^ ((w << 2) | i));
        wait_until(t_start + TC);
#pragma unroll
        for (int i = 0; i < 32; ++i) {
            if (i >= CAP) wait_vm<CAP - 1>();
            dma16(rc, vo[i & 3], sc_ + i * 4096u, ldsb + L_C + (4 * i + w) * 1024);
        }
        wait_vm<(CAP < 16 ? CAP : 16)>();
    } else {
        const int v = w - 4;
        tag32 = (unsigned)next_epoch(__hip_atomic_load((gu64*)(p.ws + WS_E), RLX_AGENT));
        __builtin_amdgcn_s_setprio(3);
        wait_until(t_start + T2);
        v4u raw[16];
        const unsigned qo = WS_H2 + b * 65536 + v * 16384 + lane * 16;
#pragma unroll
        for (int i = 0; i < 16; ++i) raw[i] = ld_sc1(rws, qo + i * 1024);
        const unsigned gg = (lane >> 3) & 3, s3 = (lane >> 1) & 3, sg = s3 ^ ((gg & 1) * 15);
        const unsigned wb = L_QA + (v >> 1) * 16384 + (s3 + 4 * (lane >> 5)) * 1024 + gg * 256 + 8 * (lane & 1);
#define Q_BAD(q_) ((raw[4 * (q_)].y ^ tag32) | (raw[4 * (q_)].w ^ tag32) | (raw[4 * (q_) + 1].y ^ tag32) | (raw[4 * (q_) + 1].w ^ tag32) | \
                   (raw[4 * (q_) + 2].y ^ tag32) | (raw[4 * (q_) + 2].w ^ tag32) | (raw[4 * (q_) + 3].y ^ tag32) | (raw[4 * (q_) + 3].w ^ tag32))
#define Q_PUT(q_) do { _Pragma("unroll") for (int i = 4 * (q_); i < 4 * (q_) + 4; ++i) \
                   *(LAS v2u*)(L + wb + (i & 1) * 8192 + 16 * ((8 * (v & 1) + (i >> 1)) ^ sg)) = v2u{raw[i].x, raw[i].z}; } while (0)
        unsigned need = 0;
#pragma unroll
        for (int q = 0; q < 4; ++q) {
            if (__builtin_amdgcn_ballot_w64(Q_BAD(q) != 0) != 0) need |= 1u << q;
            Q_PUT(q);
        }
        for (unsigned spins = 0; need != 0 && spins <= SPIN_CAP; ++spins) {
            asm volatile("" ::: "memory");
#pragma unroll
            for (int q = 0; q < 4; ++q)
                if ((need >> q) & 1) {
#pragma unroll
                    for (int i = 4 * q; i < 4 * q + 4; ++i) raw[i] = ld_sc1(rws, qo + i * 1024);
                }
#pragma unroll
            for (int q = 0; q < 4; ++q)
                if ((need >> q) & 1) {
                    if (__builtin_amdgcn_ballot_w64(Q_BAD(q) != 0) == 0) { need &= ~(1u << q); Q_PUT(q); }
                }
            if (spins > 2) __builtin_amdgcn_s_sleep(4);
        }
        __builtin_amdgcn_s_waitcnt(0x0F70);
    }
    const unsigned gsw = ((r16 & 3) << 2) | (r16 >> 2);
    unsigned ra[4], qa_[4];
#pragma unroll
    for (int i = 0; i < 4; ++i) {
        ra[i] = L_C + (16 * kq + r16) * 1024 + 16 * ((4 * g + i) ^ gsw);
        qa_[i] = L_QA + hh * 16384 + i * 1024 + g * 256 + 16 * (r16 ^ i ^ ((g & 1) * 15));
        asm volatile("" : "+v"(ra[i]), "+v"(qa_[i]));
    }
    const int head = lane & 31, kh = lane >> 5;
    const unsigned sw0 = (tq << 2) | (2 * kh), lowc = 8 * (w & 1) + 2 * (g & 1) + (tp >> 1);
    const unsigned tb = L_C + (8 * kh + tq) * 1024 + (w >> 1) * 256 + 8 * (tp & 1);
    unsigned ta[2][2], tb2[2][2];
#pragma unroll
    for (int ct = 0; ct < 2; ++ct) {
        ta[ct][0] = tb + 16 * ((lowc + 4 * ct) ^ sw0); ta[ct][1] = tb + 4096 + 16 * ((lowc + 4 * ct) ^ (sw0 | 1));
        tb2[ct][0] = ta[ct][0] + 65536; tb2[ct][1] = ta[ct][1] + 65536;
        asm volatile("" : "+v"(ta[ct][0]), "+v"(ta[ct][1]), "+v"(tb2[ct][0]), "+v"(tb2[ct][1]));
    }
    unsigned rb[4];
#pragma unroll
    for (int i = 0; i < 4; ++i) { rb[i] = ra[i] + 65536; asm volatile("" : "+v"(rb[i])); }
    __builtin_amdgcn_s_setprio(0);
    bar();
    f32x4 sa = {0.f, 0.f, 0.f, 0.f}, sc = {0.f, 0.f, 0.f, 0.f};
    v4u pf[8], cf[8][2];
#define C_READ(ks_) do { _Pragma("unroll") for (int ct = 0; ct < 2; ++ct) cf[ks_][ct] = (ks_) < 4 ? tr_frag(L, ta[ct][0] + (ks_) * 16384, ta[ct][1] + (ks_) * 16384) : tr_frag(L, tb2[ct][0] + ((ks_) - 4) * 16384, tb2[ct][1] + ((ks_) - 4) * 16384); } while (0)
    {
        v4u qf[16], ca[16];
#define S_READ(s_) do { qf[s_] = lds128(L, qa_[(s_) & 3] + ((s_) >> 2) * 4096); ca[s_] = lds128(L, ra[(s_) & 3] + ((s_) >> 2) * 256); } while (0)
#pragma unroll
        for (int s = 0; s < 6; ++s) { S_READ(s); __builtin_amdgcn_sched_barrier(0); }
#pragma unroll
        for (int s = 0; s < 16; ++s) {
            if (s + 6 < 16) S_READ(s + 6);
            if (s >= 12) C_READ(s - 12);
            __builtin_amdgcn_sched_barrier(0);
            sa = mfma16(ca[s], qf[s], sa);
            __builtin_amdgcn_sched_barrier(0);
        }
        wait_vm<0>();
        bar();
        st_sc1(rws, WS_H5 + g2 * 8192 + w * 1024 + lane * 16, v4u{SENT, SENT, SENT, SENT});
        __builtin_amdgcn_sched_barrier(0);
#pragma unroll
        for (int s = 0; s < 12; ++s) { ca[s] = lds128(L, rb[s & 3] + (s >> 2) * 256); __builtin_amdgcn_sched_barrier(0); }
#pragma unroll
        for (int s = 0; s < 16; ++s) {
            if (s + 12 < 16) ca[s + 12] = lds128(L, rb[(s + 12) & 3] + ((s + 12) >> 2) * 256);
            __builtin_amdgcn_sched_barrier(0);
            sc = mfma16(ca[s], qf[s], sc);
            __builtin_amdgcn_sched_barrier(0);
        }
    }
    {
        const float dsc = 0.08838834764831845f;
        const unsigned a0 = pk2(sa[0], sa[1]), a1 = pk2(sa[2], sa[3]), c0 = pk2(sc[0], sc[1]), c1 = pk2(sc[2], sc[3]);
        const unsigned b0 = pk2(bflo(a0) * dsc, bfhi(a0) * dsc), b1 = pk2(bflo(a1) * dsc, bfhi(a1) * dsc), d0 = pk2(bflo(c0) * dsc, bfhi(c0) * dsc), d1 = pk2(bflo(c1) * dsc, bfhi(c1) * dsc);
        sa = f32x4{bflo(b0), bfhi(b0), bflo(b1), bfhi(b1)};
        sc = f32x4{bflo(d0), bfhi(d0), bflo(d1), bfhi(d1)};
    }
    float m = fmx(fmx(fmx(sa[0], sa[1]), fmx(sa[2], sa[3])), fmx(fmx(sc[0], sc[1]), fmx(sc[2], sc[3])));
    m = max32x(max16x(m));
    if (lane < 16) *(LAS float*)(L + L_MX + (16 * hh + r16) * 16 + kq * 4) = m;
    bar();
    {
        const f32x4 mm = *(LAS f32x4*)(L + L_MX + (16 * hh + r16) * 16);
        __builtin_amdgcn_sched_barrier(0);
        C_READ(4); C_READ(5); C_READ(6);
        __builtin_amdgcn_sched_barrier(0);
        const float M = fmx(fmx(mm.x, mm.y), fmx(mm.z, mm.w));
        float pa[4], pc[4];
#pragma unroll
        for (int i = 0; i < 4; ++i) { pa[i] = __builtin_amdgcn_exp2f((sa[i] - M) * LOG2E); pc[i] = __builtin_amdgcn_exp2f((sc[i] - M) * LOG2E); }
        const unsigned po = L_P + (16 * hh + r16) * 256 + 8 * (g & 1);
        *(LAS v2u*)(L + po + 16 * ((2 * kq + (g >> 1)) ^ r16)) = v2u{pk2(pa[0], pa[1]), pk2(pa[2], pa[3])};
        *(LAS v2u*)(L + po + 16 * ((8 + 2 * kq + (g >> 1)) ^ r16)) = v2u{pk2(pc[0], pc[1]), pk2(pc[2], pc[3])};
        const float l = sum32x(sum16x(((pa[0] + pa[1]) + (pa[2] + pa[3])) + ((pc[0] + pc[1]) + (pc[2] + pc[3]))));
        if (lane < 16) *(LAS float*)(L + L_LS + (16 * hh + r16) * 16 + kq * 4) = l;
        if (w == 4 && lane == 0) *(LAS unsigned*)(L + L_CK) = tag32;
    }
    wait_vm<0>();
    bar();
    {
        f32x16 acc[2];
#pragma unroll
        for (int i = 0; i < 16; ++i) { acc[0][i] = 0.f; acc[1][i] = 0.f; }
        const unsigned pa_ = L_P + head * 256;
#define P_READ(ks_) do { pf[ks_] = lds128(L, pa_ + 16 * ((2 * (ks_) + kh) ^ r16)); } while (0)
        P_READ(0); P_READ(1); P_READ(2);
        __builtin_amdgcn_sched_barrier(0);
        const unsigned sto = (w == 4 && lane < 32) ? WS_ST + (head >> 2) * 16384 + g2 * 128 + (head & 3) * 16 : OOB;
        f32x4 mm, ll;
        const unsigned key = ck_key(*(LAS unsigned*)(L + L_CK), chunk & 3);
        const unsigned cko = (lane & 7) == 0 ? WS_CK + g2 * 1024 + w * 128 + (lane >> 3) * 16 : OOB;
#pragma unroll
        for (int ks = 0; ks < 8; ++ks) {
            if (ks == 1) { mm = *(LAS f32x4*)(L + L_MX + head * 16); ll = *(LAS f32x4*)(L + L_LS + head * 16); __builtin_amdgcn_sched_barrier(0); }
            if (ks + 3 < 8) P_READ(ks + 3);
            if (ks == 0) C_READ(7);
            __builtin_amdgcn_sched_barrier(0);
            acc[0] = mfma32(cf[ks][0], pf[ks], acc[0]); acc[1] = mfma32(cf[ks][1], pf[ks], acc[1]);
            __builtin_amdgcn_sched_barrier(0);
            if (ks == 2) {
                st_sc1(rws, sto, v4u{__float_as_uint(fmx(fmx(mm.x, mm.y), fmx(mm.z, mm.w))), tag32, __float_as_uint((ll.x + ll.y) + (ll.z + ll.w)), tag32});
                __builtin_amdgcn_sched_barrier(0);
            }
        }
        const unsigned sgw = L_C + head * 1024 + (w >> 1) * 256 + 128 * ((w & 1) ^ ((head >> 1) & 1)) + 8 * kh;
#pragma unroll
        for (int ct = 0; ct < 2; ++ct)
#pragma unroll
            for (int q = 0; q < 4; ++q)
                *(LAS v2u*)(L + sgw + 16 * ((4 * ct + q) ^ (head & 7))) = v2u{pk2(acc[ct][4 * q], acc[ct][4 * q + 1]), pk2(acc[ct][4 * q + 2], acc[ct][4 * q + 3])};
        const int hr = lane >> 3;
        const unsigned sgr = L_C + hr * 1024 + (w >> 1) * 256 + 128 * ((w & 1) ^ ((hr >> 1) & 1)) + 16 * ((lane & 7) ^ hr);
        const unsigned go = WS_H3 + (hr * 4 + kq) * 32768 + (b * 32 + chunk) * 256 + hh * 128 + (lane & 7) * 16;
        v4u ov[4];
#pragma unroll
        for (int i = 0; i < 4; ++i) { ov[i] = lds128(L, sgr + i * 8192); st_sc1(rws, go + i * (8 * 4 * 32768), ov[i]); }
        st_sc1(rws, cko, v4u{xor8(ov[0]), xor8(ov[1]), xor8(ov[2]), xor8(ov[3])} ^ key);
    }
}

__global__ void __launch_bounds__(512, 2) mla_mlp_decode(Params p) {
    __shared__ __attribute__((aligned(16))) unsigned char smem[LDS_BYTES];
    LAS unsigned char* L = (LAS unsigned char*)smem;
    const int w = __builtin_amdgcn_readfirstlane(threadIdx.x >> 6), lane = threadIdx.x & 63;
    const int xc = blockIdx.x & 7, k = blockIdx.x >> 3;
    if (k < 16) role_g1(p, L, xc, k, w, lane);
    else role_g2(p, L, xc, k, w, lane);
}

}

extern "C" void kernel_launch(void* const* d_in, const int* in_sizes, int n_in, void* d_out, int out_size, void* d_ws, size_t ws_size,
                              hipStream_t stream) {
    Params p;
    p.x = (const u16*)d_in[0];
    p.ckv = (const u16*)d_in[1];
    p.wq = (const u16*)d_in[2];
    p.wuk = (const u16*)d_in[3];
    p.wuv = (const u16*)d_in[4];
    p.wo = (const u16*)d_in[5];
    p.w1 = (const u16*)d_in[7];
    p.b1 = (const u16*)d_in[8];
    p.w2 = (const u16*)d_in[9];
    p.b2 = (const u16*)d_in[10];
    p.out = (u16*)d_out;
    p.ws = (unsigned char*)d_ws;
    hipLaunchKernelGGL(mla_mlp_decode, dim3(256), dim3(512), 0, stream, p);
}
```
